```python
import torch

def _softmax(x, axis=-1):
    return torch.softmax(x, dim=axis)

def _gelu(x):
    return torch.nn.functional.gelu(x, approximate='tanh')

def _silu(x):
    return torch.nn.functional.silu(x)

def _sigmoid(x):
    return torch.sigmoid(x)

def _layernorm(x, axis=-1, eps=1e-05):
    m = x.mean(dim=axis, keepdim=True)
    v = x.var(dim=axis, keepdim=True, unbiased=False)
    return (x - m) / torch.sqrt(v + eps)

def _scatter_add(tgt, idx, src, axis=0):
    return tgt.clone().index_add_(axis, idx, src)

def _segment_sum(src, seg, n):
    out = torch.zeros((n,) + src.shape[1:], dtype=src.dtype)
    return out.index_add_(0, seg, src)

def _pad(x, pw):
    flat = tuple((v for pair in reversed(pw) for v in pair))
    return torch.nn.functional.pad(x, flat)
kernel_name = 'interleave_layers__memory_aug_recurrent_mem_tr__grouped_kv_mqa__prompt'

def setup_inputs():

    _w = lambda a: (lambda t: t.to(torch.bfloat16) if t.is_floating_point() else t)(torch.as_tensor(a))

    def _A_setup():
        B, S, Dm = (1, 2048, 448)
        H, Dh = (16, 256)
        M, seg = (8, 32)
        seg = min(seg, S)
        S = S // seg * seg
        return {'x': _w((lambda *s: torch.randn(*s, dtype=torch.bfloat16) * 0.05)(B, S, Dm)), 'mem0': _w((lambda *s: torch.randn(*s, dtype=torch.bfloat16) * 0.2)(B, M, Dm)), 'Wq': _w((lambda *s: torch.randn(*s, dtype=torch.bfloat16) * 1.6)(Dm, H, Dh) * Dm ** (-0.5)), 'Wk': _w((lambda *s: torch.randn(*s, dtype=torch.bfloat16) * 1.6)(Dm, H, Dh) * Dm ** (-0.5)), 'Wv': _w((lambda *s: torch.randn(*s, dtype=torch.bfloat16) * 0.2)(Dm, H, Dh) * Dm ** (-0.5)), 'Wo': _w((lambda *s: torch.randn(*s, dtype=torch.bfloat16) * 0.2)(H, Dh, Dm) * (H * Dh) ** (-0.5)), 'seg_len': seg}
    _A_in = _A_setup()

    def _B_setup():
        B, S, D, H = (1, 2048, 448, 16)
        Dh = D // H
        return {'x': _w((lambda *s: torch.randn(*s, dtype=torch.bfloat16) * 0.05)(B, S, D)), 'wq': _w((lambda *s: torch.randn(*s, dtype=torch.bfloat16) * 0.05)(D, H * Dh)), 'wk': _w((lambda *s: torch.randn(*s, dtype=torch.bfloat16) * 0.05)(D, Dh)), 'wv': _w((lambda *s: torch.randn(*s, dtype=torch.bfloat16) * 0.05)(D, Dh)), 'wo': _w((lambda *s: torch.randn(*s, dtype=torch.bfloat16) * 0.05)(H * Dh, D))}
    _B_in = _B_setup()
    return {'A_x': _A_in['x'], 'A_mem0': _A_in['mem0'], 'A_Wq': _A_in['Wq'], 'A_Wk': _A_in['Wk'], 'A_Wv': _A_in['Wv'], 'A_Wo': _A_in['Wo'], 'A_seg_len': _A_in['seg_len'], 'B_x': _B_in['x'], 'B_wq': _B_in['wq'], 'B_wk': _B_in['wk'], 'B_wv': _B_in['wv'], 'B_wo': _B_in['wo']}

def reference(A_x, A_mem0, A_Wq, A_Wk, A_Wv, A_Wo, A_seg_len, B_x, B_wq, B_wk, B_wv, B_wo):

    def _A_ref(x, mem0, Wq, Wk, Wv, Wo, seg_len):
        B, S, Dm = x.shape
        H, Dh = (Wq.shape[1], Wq.shape[2])
        M = mem0.shape[1]
        scale = Dh ** (-0.5)
        n_seg = S // seg_len
        L = M + seg_len
        idx = torch.arange(L)
        mask = (idx[:, None] < M) | (idx[:, None] >= idx[None, :])
        mem = mem0
        outs = []
        for s in range(n_seg):
            xs = x[:, s * seg_len:(s + 1) * seg_len, :].clone()
            h = torch.concatenate([mem, xs], axis=1)
            q = torch.einsum('btd,dhe->bhte', h, Wq)
            k = torch.einsum('btd,dhe->bhte', h, Wk)
            v = torch.einsum('btd,dhe->bhte', h, Wv)
            scores = torch.einsum('bhqe,bhke->bhqk', q, k) * scale
            scores = torch.where(mask[None, None], scores, -1000000000.0)
            p = _softmax(scores, axis=-1)
            o = torch.einsum('bhqk,bhke->bhqe', p, v)
            o = torch.einsum('bhte,hed->btd', o, Wo)
            mem = o[:, :M, :].clone()
            outs.append(o[:, M:, :])
        return torch.concatenate(outs, axis=1)

    def _B_ref(x, wq, wk, wv, wo):
        B, S, D = x.shape
        H = 16
        Dh = D // H
        q = (lambda x, *d: x.permute(*d))((x @ wq).reshape(B, S, H, Dh), 0, 2, 1, 3)
        k = x @ wk
        v = x @ wv
        scores = torch.einsum('bhqd,bkd->bhqk', q, k) * Dh ** (-0.5)
        p = _softmax(scores, axis=-1)
        o = torch.einsum('bhqk,bkd->bhqd', p, v)
        return (lambda x, *d: x.permute(*d))(o, 0, 2, 1, 3).reshape(B, S, H * Dh) @ wo
    x = A_x
    x = x + _A_ref(_layernorm(x, axis=-1), A_mem0, A_Wq, A_Wk, A_Wv, A_Wo, A_seg_len)
    x = x + _B_ref(_layernorm(x, axis=-1), B_wq, B_wk, B_wv, B_wo)
    x = x + _A_ref(_layernorm(x, axis=-1), A_mem0, A_Wq, A_Wk, A_Wv, A_Wo, A_seg_len)
    x = x + _B_ref(_layernorm(x, axis=-1), B_wq, B_wk, B_wv, B_wo)
    return x
```

```cpp
#include <hip/hip_runtime.h>
#include <hip/hip_bf16.h>
#include <stdint.h>

typedef short bf16x8 __attribute__((ext_vector_type(8)));
typedef short bf16x4 __attribute__((ext_vector_type(4)));
typedef float f32x4 __attribute__((ext_vector_type(4)));
typedef float f32x2 __attribute__((ext_vector_type(2)));
typedef float f32x16 __attribute__((ext_vector_type(16)));
typedef unsigned u32x4 __attribute__((ext_vector_type(4)));
typedef unsigned u32x2 __attribute__((ext_vector_type(2)));
typedef __bf16 bfv2 __attribute__((ext_vector_type(2)));
typedef unsigned short u16;
#define DI __device__ __forceinline__
#define GAS __attribute__((address_space(1)))
#define LAS __attribute__((address_space(3)))
typedef GAS unsigned gu32;
#define RLX_AGENT __ATOMIC_RELAXED, __HIP_MEMORY_SCOPE_AGENT

constexpr int SQ = 2048, DM = 448, NH = 16, NSEG = 64, KS = 14, NT = 28;
constexpr float LOG2E = 1.4426950408889634f;
constexpr float A_SCALE = 0.0625f * LOG2E;
constexpr float B_SCALE = 0.18898223650461362f * LOG2E;
constexpr unsigned SENT = 0xFFFFFFFFu;

constexpr size_t WS_CTL = 0;
constexpr size_t CTL_BYTES = 163840;
constexpr size_t WS_AF = 163840;
constexpr size_t FRAGW = (size_t)NH * NT * KS * 1024;
constexpr size_t WS_BF = WS_AF + FRAGW;
constexpr size_t WS_XF = WS_BF + FRAGW;
constexpr size_t XFB = (size_t)NSEG * 56 * 32 * 16;
constexpr size_t WS_GF = WS_XF + XFB;
constexpr size_t GFB = (size_t)NSEG * NH * 2 * KS * 1024;
constexpr size_t WS_UF = WS_GF + GFB;
constexpr size_t WS_GMF = WS_UF + GFB;
constexpr size_t GMFB = (size_t)NSEG * NH * 7168;
constexpr size_t WS_T2F = WS_GMF + 2 * GMFB;
constexpr size_t WS_PF = WS_T2F + 2 * GMFB;
constexpr size_t PFB = (size_t)NSEG * 8 * 2 * 2560;
constexpr size_t WS_X1 = WS_PF + 2 * PFB;
constexpr size_t XB = (size_t)SQ * DM * 2;
constexpr size_t WS_X2 = WS_X1 + 2 * XB;
constexpr size_t WS_QKV = WS_X2 + XB;
constexpr size_t QFB = (size_t)NH * SQ * 64;
constexpr size_t KFB = (size_t)64 * 2048;
constexpr size_t QKV_Q = 0, QKV_K = QFB, QKV_V = QFB + KFB, QKV_KN = QFB + 2 * KFB, QKVB = QFB + 2 * KFB + 4096;
constexpr size_t WS_OF = WS_QKV + 2 * QKVB;
constexpr size_t OFB = (size_t)NSEG * NH * 2048;
constexpr size_t WS_WQF = WS_OF + OFB;
constexpr size_t WQFB = (size_t)NH * 2 * KS * 1024;
constexpr size_t WS_WKF = WS_WQF + WQFB;
constexpr size_t WS_WVF = WS_WKF + 2 * KS * 1024;
constexpr size_t WS_WOF = WS_WVF + 2 * KS * 1024;
constexpr size_t WOFB = (size_t)NT * NH * 1024;
constexpr size_t WS_END0 = WS_WOF + WOFB;
constexpr size_t WS_SLOT = (WS_END0 + (2u << 20) - 1) / (2u << 20) * (2u << 20);
constexpr int HINT_OFF = 7424;
constexpr int BPS = 4;
constexpr int NBLK = NSEG * BPS;
constexpr size_t SLOT_LAYER = (size_t)NBLK * 16384;
constexpr size_t WS_END = WS_SLOT + 2 * SLOT_LAYER;
static_assert(WS_END <= (size_t)256 << 20, "workspace");
constexpr int CTL_TICKET_CAL = 0;
constexpr int CTL_ROLE = 64;
constexpr int CTL_TMO = 512;
constexpr int CTL_TBL = 1024;
constexpr size_t CTL_PROG = 16384, PROG_LAYER = 416 * 128, CTL_FIN = 131072;
constexpr int PG_GU = 0, PG_SP = 64, PG_O = 128, PG_XP = 192, PG_XO = 256, PG_LN = 320, PG_ATT = 324, PG_B3 = 356, PG_B1Q = 364, PG_B1K = 372;

DI unsigned cvtpk(float a, float b) { f32x2 v = {a, b}; bfv2 r = __builtin_convertvector(v, bfv2); return __builtin_bit_cast(unsigned, r); }
DI float bf2f(u16 x) { return __uint_as_float((unsigned)x << 16); }
DI float bflo(unsigned x) { return __uint_as_float(x << 16); }
DI float bfhi(unsigned x) { return __uint_as_float(x & 0xffff0000u); }
DI bf16x8 asb8(u32x4 v) { return __builtin_bit_cast(bf16x8, v); }
DI bf16x4 asb4(u32x2 v) { return __builtin_bit_cast(bf16x4, v); }
DI f32x4 mfma32(u32x4 a, u32x4 b, f32x4 c) { return __builtin_amdgcn_mfma_f32_16x16x32_bf16(asb8(a), asb8(b), c, 0, 0, 0); }
DI f32x4 mfma16(u32x2 a, u32x2 b, f32x4 c) { return __builtin_amdgcn_mfma_f32_16x16x16bf16_1k(asb4(a), asb4(b), c, 0, 0, 0); }
DI f32x16 mfmaL(u32x4 a, u32x4 b, f32x16 c) { return __builtin_amdgcn_mfma_f32_32x32x16_bf16(asb8(a), asb8(b), c, 0, 0, 0); }
DI u32x4 pack8(f32x4 a, f32x4 b) { u32x4 r; r.x = cvtpk(a[0], a[1]); r.y = cvtpk(a[2], a[3]); r.z = cvtpk(b[0], b[1]); r.w = cvtpk(b[2], b[3]); return r; }
DI float xg_max(float x) {
  auto s = __builtin_amdgcn_permlane16_swap(__float_as_uint(x), __float_as_uint(x), false, false);
  x = fmaxf(__uint_as_float(s[0]), __uint_as_float(s[1]));
  auto t = __builtin_amdgcn_permlane32_swap(__float_as_uint(x), __float_as_uint(x), false, false);
  return fmaxf(__uint_as_float(t[0]), __uint_as_float(t[1]));
}
DI float xg_sum(float x) {
  auto s = __builtin_amdgcn_permlane16_swap(__float_as_uint(x), __float_as_uint(x), false, false);
  x = __uint_as_float(s[0]) + __uint_as_float(s[1]);
  auto t = __builtin_amdgcn_permlane32_swap(__float_as_uint(x), __float_as_uint(x), false, false);
  return __uint_as_float(t[0]) + __uint_as_float(t[1]);
}
DI float ex2(float x) { return __builtin_amdgcn_exp2f(x); }
DI unsigned xcc_id() { return (unsigned)__builtin_amdgcn_s_getreg((3 << 11) | 20) & 0xFu; }
DI void lbar() { asm volatile("s_waitcnt lgkmcnt(0)\n\ts_barrier" ::: "memory"); }

#define SB0() __builtin_amdgcn_sched_barrier(0)
struct PrepArgs {
  const u16 *x, *mem0, *Wq, *Wk, *Wv, *Wo, *wq, *wk, *wv, *wo;
  char* ws;
};
typedef short s16x4 __attribute__((ext_vector_type(4)));
DI u32x2 lds_tr(const char* p) { return __builtin_bit_cast(u32x2, __builtin_amdgcn_ds_read_tr16_b64_v4i16((LAS s16x4*)p)); }
#define FILL_AUX 16
#define FRAG_AUX 16
constexpr int WO_RS = 144;

__global__ void __launch_bounds__(512, 2) k_prep(PrepArgs a) {
  __shared__ __attribute__((aligned(16))) char sbuf[256 * WO_RS + 8 * 8192];
  const int tid = threadIdx.x, lane = tid & 63, wave = __builtin_amdgcn_readfirstlane(tid >> 6);
  const int g = lane >> 4, rho = lane & 15;
  const int b = blockIdx.x;
#define PSP(i) {}
  PSP(0)
  auto fill = [&]() {
#pragma unroll
    for (int q = 0; q < 2; ++q) {
      const int bi = b * 2 + q;
      const __amdgpu_buffer_rsrc_t rf = __builtin_amdgcn_make_buffer_rsrc((void*)(a.ws + WS_SLOT + (size_t)bi * 16384), 0, 16384, 0x00020000);
      const bool m0 = (bi % NBLK) == 0;
#pragma unroll
      for (int n = 0; n < 2; ++n) {
        const int c = tid + 512 * n, cc = c & 511;
        u32x4 v = {SENT, SENT, SENT, SENT};
        if (m0 && cc < 448) v = *(const u32x4*)(a.mem0 + (size_t)(cc & 7) * DM + (cc >> 3) * 8);
        if (m0 && cc == HINT_OFF / 16) v = u32x4{1u, 1u, 1u, 1u};
        __builtin_amdgcn_raw_buffer_store_b128(v, rf, (unsigned)c * 16, 0, FILL_AUX);
      }
    }
  };
  u32x4 st[9];
  {
#pragma unroll
    for (int n = 0; n < 7; ++n) {
      const int c = tid + 512 * n, t4 = c / 896, cc = c % 896;
      const u16* src = t4 == 0 ? a.Wq : (t4 == 1 ? a.Wk : (t4 == 2 ? a.Wv : a.Wo));
      st[n] = ((const u32x4*)src)[(size_t)b * 896 + cc];
    }
    st[7] = ((const u32x4*)a.x)[(size_t)b * 448 + min(tid, 447)];
    st[8] = ((const u32x4*)(tid < 256 ? a.wq : a.wo))[(size_t)b * 98 + min(tid & 255, 97)];
#pragma unroll
    for (int n = 0; n < 9; ++n) asm volatile("" :: "v"(st[n]));
  }
  if (b < 224) {
    const int mh = (b & 7) * 4 + (b >> 3) / 7, grp = (b >> 3) % 7;
    const int mat = mh >> 4, h = mh & 15;
    const u16* rowW = mat == 0 ? a.Wk : a.Wv;
    u32x4 rv[2][4][4];
#pragma unroll
    for (int n = 0; n < 2; ++n) {
      const int v = min(wave + 8 * n, KS - 1);
#pragma unroll
      for (int q4 = 0; q4 < 4; ++q4)
#pragma unroll
        for (int i = 0; i < 4; ++i) rv[n][q4][i] = *(const u32x4*)(rowW + ((size_t)(32 * v + 8 * i + (lane >> 3)) * NH + h) * 256 + 64 * q4 + 8 * (lane & 7));
    }
    if (mat == 0) {
#pragma unroll
      for (int n = 0; n < 4; ++n) {
        const int c = tid + 512 * n, i = c >> 9, s = (c >> 6) & 7, l = c & 63;
        const int t = grp * 4 + i, u = t >> 1, p = t & 1, r = l & 15;
        const int aa = 32 * u + 8 * (r >> 2) + 4 * p + (r & 3);
        ((u32x4*)sbuf)[c] = *(const u32x4*)(a.Wq + ((size_t)aa * NH + h) * 256 + 32 * s + 8 * (l >> 4));
      }
    } else {
#pragma unroll
      for (int n = 0; n < 4; ++n) {
        const int c = tid + 512 * n, e = c >> 3, cc = c & 7;
        *(u32x4*)(sbuf + e * WO_RS + cc * 16) = *(const u32x4*)(a.Wo + ((size_t)h * 256 + e) * DM + grp * 64 + cc * 8);
      }
    }
    __syncthreads();
    fill();
    PSP(1)
    const __amdgpu_buffer_rsrc_t rdst = __builtin_amdgcn_make_buffer_rsrc((void*)(a.ws + (mat == 0 ? WS_AF : WS_BF)), 0, (unsigned)FRAGW, 0x00020000);
    const float sc = mat == 0 ? A_SCALE : 1.0f;
    u32x4* rt = (u32x4*)(sbuf + 256 * WO_RS) + wave * 512;
    f32x4 acc[2][4][2];
#pragma unroll
    for (int n = 0; n < 2; ++n)
#pragma unroll
      for (int i = 0; i < 4; ++i) { acc[n][i][0] = f32x4{0, 0, 0, 0}; acc[n][i][1] = f32x4{0, 0, 0, 0}; }
#pragma unroll
    for (int q4 = 0; q4 < 4; ++q4) {
#pragma unroll
      for (int n = 0; n < 2; ++n)
#pragma unroll
        for (int i = 0; i < 4; ++i) { const int row = 8 * i + (lane >> 3); rt[n * 256 + row * 8 + ((lane & 7) ^ (row & 7))] = rv[n][q4][i]; }
#pragma unroll
      for (int s2 = 0; s2 < 2; ++s2) {
        const int s = 2 * q4 + s2;
        const int r0 = 8 * (rho >> 2) + (rho & 3), r1 = r0 + 4;
        u32x4 f[2][2];
#pragma unroll
        for (int n = 0; n < 2; ++n) { f[n][0] = rt[n * 256 + r0 * 8 + ((4 * s2 + g) ^ (r0 & 7))]; f[n][1] = rt[n * 256 + r1 * 8 + ((4 * s2 + g) ^ (r1 & 7))]; }
#pragma unroll
        for (int i = 0; i < 4; ++i) {
          u32x4 cw;
          if (mat == 0) cw = ((const u32x4*)sbuf)[(i * 8 + s) * 64 + lane];
          else {
            const char* pp = sbuf + (32 * s + 8 * g + (rho >> 2)) * WO_RS + (i * 16 + 4 * (rho & 3)) * 2;
            const u32x2 lo = lds_tr(pp), hi = lds_tr(pp + 4 * WO_RS);
            cw = u32x4{lo.x, lo.y, hi.x, hi.y};
          }
#pragma unroll
          for (int n = 0; n < 2; ++n) { acc[n][i][0] = mfma32(f[n][0], cw, acc[n][i][0]); acc[n][i][1] = mfma32(f[n][1], cw, acc[n][i][1]); }
        }
      }
    }
    PSP(2)
#pragma unroll
    for (int n = 0; n < 2; ++n) {
      const int v = wave + 8 * n;
      if (v < KS) {
#pragma unroll
        for (int i = 0; i < 4; ++i)
          __builtin_amdgcn_raw_buffer_store_b128(pack8(acc[n][i][0] * sc, acc[n][i][1] * sc), rdst, (unsigned)((((h * NT + grp * 4 + i) * KS + v) * 64 + lane) * 16), 0, FRAG_AUX);
      }
    }
    PSP(3)
    return;
  }
  {
    const int bw = b - 224;
    fill();
#pragma unroll
    for (int n = 0; n < 4; ++n) {
      const int c = (bw * 4 + n) * 512 + tid;
      u16 tmp[8];
      u32x4* dst;
      const int l = c & 63, gg = l >> 4, rr = l & 15;
      if (c < 28672) {
        const int f = c >> 6, ks = f % KS, p = (f / KS) & 1, h = f / (2 * KS);
        const int d = 8 * (rr >> 2) + 4 * p + (rr & 3);
#pragma unroll
        for (int j = 0; j < 8; ++j) tmp[j] = d < 28 ? a.wq[(size_t)(32 * ks + 8 * gg + j) * DM + 28 * h + d] : (u16)0;
        dst = (u32x4*)(a.ws + WS_WQF) + c;
      } else if (c < 28672 + 1792) {
        const int cc = c - 28672, f = cc >> 6, ks = f % KS, p = f / KS;
        const int d = 8 * (rr >> 2) + 4 * p + (rr & 3);
#pragma unroll
        for (int j = 0; j < 8; ++j) tmp[j] = d < 28 ? a.wk[(size_t)(32 * ks + 8 * gg + j) * 28 + d] : (u16)0;
        dst = (u32x4*)(a.ws + WS_WKF) + cc;
      } else if (c < 28672 + 3584) {
        const int cc = c - 28672 - 1792, f = cc >> 6, ks = f % KS, dt = f / KS;
        const int d = 16 * dt + rr;
#pragma unroll
        for (int j = 0; j < 8; ++j) tmp[j] = d < 28 ? a.wv[(size_t)(32 * ks + 8 * gg + j) * 28 + d] : (u16)0;
        dst = (u32x4*)(a.ws + WS_WVF) + cc;
      } else if (c < 28672 + 3584 + 28672) {
        const int cc = c - 28672 - 3584, f = cc >> 6, h = f % NH, ct = f / NH;
#pragma unroll
        for (int j = 0; j < 8; ++j) {
          const int d = 16 * (gg >> 1) + 8 * (j >> 2) + 4 * (gg & 1) + (j & 3);
          tmp[j] = d < 28 ? a.wo[(size_t)(28 * h + d) * DM + 16 * ct + rr] : (u16)0;
        }
        dst = (u32x4*)(a.ws + WS_WOF) + cc;
      } else continue;
      u32x4 v; v.x = tmp[0] | ((unsigned)tmp[1] << 16); v.y = tmp[2] | ((unsigned)tmp[3] << 16); v.z = tmp[4] | ((unsigned)tmp[5] << 16); v.w = tmp[6] | ((unsigned)tmp[7] << 16);
      *dst = v;
    }
    PSP(1)
    if (bw == 0) { for (int i = tid; i < CTL_TBL; i += 512) ((unsigned*)(a.ws + WS_CTL))[i] = 0u; }
    if (bw == 3) { for (int i = tid; i < 256; i += 512) ((unsigned*)(a.ws + WS_CTL + CTL_FIN))[i * 32] = 0u; }
    if (bw >= 1 && bw <= 2) { for (int i = tid; i < 416; i += 512) ((unsigned*)(a.ws + WS_CTL + CTL_PROG + (bw - 1) * PROG_LAYER))[i * 32] = 0u; }
    if (wave < 4) {
      const int e = bw * 4 + wave;
      const char* p0 = a.ws + WS_SLOT + (size_t)e * (BPS * 16384) + 8064;
      const char* p1 = p0 + 8192;
      unsigned best0 = 1u << 30, best1 = 1u << 30;
      for (int rep = 0; rep < 2; ++rep) {
        unsigned v; unsigned long long t0, t1, t2;
        const char* q0 = p0 - rep * 128; const char* q1 = p1 - rep * 128;
        asm volatile("s_memtime %0\n\ts_waitcnt lgkmcnt(0)" : "=s"(t0) :: "memory");
        asm volatile("global_load_dword %0, %1, off sc1\n\ts_waitcnt vmcnt(0)" : "=&v"(v) : "v"(q0) : "memory");
        asm volatile("s_memtime %0\n\ts_waitcnt lgkmcnt(0)" : "=s"(t1) :: "memory");
        asm volatile("global_load_dword %0, %1, off sc1\n\ts_waitcnt vmcnt(0)" : "=&v"(v) : "v"(q1) : "memory");
        asm volatile("s_memtime %0\n\ts_waitcnt lgkmcnt(0)" : "=s"(t2) :: "memory");
        best0 = min(best0, (unsigned)(t1 - t0)); best1 = min(best1, (unsigned)(t2 - t1));
      }
      const unsigned bit = (best1 < best0 ? 1u : 0u) ^ (xcc_id() < 4 ? 0u : 1u);
      if (lane == 0) ((unsigned*)(a.ws + WS_CTL))[CTL_TBL + e] = bit;
    }
    PSP(3)
  }
}

struct LayerArgs {
  char* ws;
  const u16* x0;
  int sleepS, sleepO, sleepP, sleepX;
  unsigned* trace;
};
#define NLAYERS 2
constexpr int HEAD_BLK = 1152;
constexpr int SPINCAP = 1 << 14;
constexpr int N_O = 28, N_SP = 64, N_SX = 16;
constexpr int NCHAIN = N_O + N_SP + N_SX;
constexpr int N_GU = 56, N_ATT = 32, N_XP = 16, N_XO = 16, N_B3 = 8, N_B1 = 12, N_LN = 4;
constexpr int W_ATT = N_GU, W_XP = W_ATT + N_ATT, W_XO = W_XP + N_XP, W_B3 = W_XO + N_XO, W_B1 = W_B3 + N_B3, W_LN = W_B1 + N_B1;
constexpr int NWORK = W_LN + N_LN;
typedef __amdgpu_buffer_rsrc_t rsrc_t;
DI rsrc_t mkrs(const void* p, size_t bytes) { return __builtin_amdgcn_make_buffer_rsrc((void*)p, 0, (unsigned)bytes, 0x00020000); }
DI u32x4 bld4(rsrc_t r, unsigned vo, unsigned so) { return __builtin_amdgcn_raw_buffer_load_b128(r, vo, so, 0); }
DI u32x4 bld4s(rsrc_t r, unsigned vo, unsigned so) { return __builtin_amdgcn_raw_buffer_load_b128(r, vo, so, 16); }
DI void bst4s(u32x4 v, rsrc_t r, unsigned vo, unsigned so) {
  __builtin_amdgcn_raw_buffer_store_b128(v, r, vo, so, 16);
  asm volatile("s_nop 1" :: "v"(v.x), "v"(v.y), "v"(v.z), "v"(v.w));
}
DI void drain() { asm volatile("s_waitcnt vmcnt(0)" ::: "memory"); }

DI void glds16(const void* gsrc, unsigned lds_base) {
  unsigned keep;
  asm volatile("s_mov_b32 %0, m0\n\ts_mov_b32 m0, %2\n\ts_nop 0\n\tglobal_load_lds_dwordx4 %1, off\n\ts_mov_b32 m0, %0" : "=&s"(keep) : "v"(gsrc), "s"(lds_base) : "memory");
}
DI void glds16s(const void* gsrc, unsigned lds_base) {
  unsigned keep;
  asm volatile("s_mov_b32 %0, m0\n\ts_mov_b32 m0, %2\n\ts_nop 0\n\tglobal_load_lds_dwordx4 %1, off sc1\n\ts_mov_b32 m0, %0" : "=&s"(keep) : "v"(gsrc), "s"(lds_base) : "memory");
}

DI bool valid4(u32x4 v) { return max(max(v.x, v.y), max(v.z, v.w)) != SENT; }
DI void snooze(int n) { for (int i = 0; i < n; ++i) __builtin_amdgcn_s_sleep(1); }

DI int wait_prog(rsrc_t rc, unsigned off, int n, int need, int lane, int& dead, int nap) {
  unsigned v;
  int spins = 0;
  for (;;) {
    v = __builtin_amdgcn_raw_buffer_load_b32(rc, (unsigned)min(lane, n - 1) * 128, off * 32, 16);
    if (__builtin_amdgcn_ballot_w64(v < (unsigned)need) == 0) break;
    if (dead || ++spins > SPINCAP) { dead = 1; return 64; }
    snooze(nap);
    asm volatile("" ::: "memory");
  }
  int r = need;
#pragma unroll
  for (int b = 32; b > 0; b >>= 1) if (__builtin_amdgcn_ballot_w64(v < (unsigned)(r + b)) == 0) r += b;
  return r;
}
DI unsigned prog_request(rsrc_t rc, unsigned off, int n, int lane) { return __builtin_amdgcn_raw_buffer_load_b32(rc, (unsigned)min(lane, n - 1) * 128, off * 32, 16); }
DI int prog_min(unsigned v, int known) {
  int r = known;
#pragma unroll
  for (int b = 32; b > 0; b >>= 1) if (__builtin_amdgcn_ballot_w64(v < (unsigned)(r + b)) == 0) r += b;
  return r;
}
DI void set_prog(rsrc_t rc, unsigned off, unsigned val) { __builtin_amdgcn_raw_buffer_store_b32(val, rc, 0, off * 32, 16); }

DI int gather_m(rsrc_t rs, unsigned soff, int tid, u32x4* lm, int& dead) {
  const unsigned voff = (unsigned)tid * 16;
  u32x4 v;
  int spins = 0;
  for (;;) {
    v = __builtin_amdgcn_raw_buffer_load_b128(rs, voff, soff, 16);
    if (__builtin_amdgcn_ballot_w64(!valid4(v)) == 0) break;
    if (dead || ++spins > SPINCAP) { dead = 1; break; }
    asm volatile("" ::: "memory");
  }
  lm[tid] = v;
  return spins;
}

#define TRDECL
#define TRC(i)
#define TRV(i, v) (void)(v);
#define TRFLUSH
#define SPAN(i) {}
DI void wait_bell_bar(const char* bell, int dl, int& dead) {
  unsigned p0, p1, p2, p3, cnt = dead ? 1u : 8192u, nap = (unsigned)dl;
  asm volatile(
      "global_load_dword %[p0], %[a], off sc1\n\t"
      "global_load_dword %[p1], %[a], off sc1\n\t"
      "global_load_dword %[p2], %[a], off sc1\n"
      "1:\n\t"
      "global_load_dword %[p3], %[a], off sc1\n\ts_waitcnt vmcnt(3)\n\tv_cmp_ne_u32 vcc, -1, %[p0]\n\ts_cbranch_vccnz 2f\n\t"
      "global_load_dword %[p0], %[a], off sc1\n\ts_waitcnt vmcnt(3)\n\tv_cmp_ne_u32 vcc, -1, %[p1]\n\ts_cbranch_vccnz 2f\n\t"
      "global_load_dword %[p1], %[a], off sc1\n\ts_waitcnt vmcnt(3)\n\tv_cmp_ne_u32 vcc, -1, %[p2]\n\ts_cbranch_vccnz 2f\n\t"
      "global_load_dword %[p2], %[a], off sc1\n\ts_waitcnt vmcnt(3)\n\tv_cmp_ne_u32 vcc, -1, %[p3]\n\ts_cbranch_vccnz 2f\n\t"
      "s_sub_u32 %[cnt], %[cnt], 1\n\ts_cmp_lg_u32 %[cnt], 0\n\ts_cbranch_scc1 1b\n"
      "2:\n\t"
      "s_cmp_eq_u32 %[nap], 0\n\ts_cbranch_scc1 4f\n"
      "3:\n\t"
      "s_sleep 1\n\ts_sub_u32 %[nap], %[nap], 1\n\ts_cmp_lg_u32 %[nap], 0\n\ts_cbranch_scc1 3b\n"
      "4:\n\t"
      "s_waitcnt lgkmcnt(0)\n\ts_barrier\n\ts_waitcnt vmcnt(0)"
      : [p0] "=&v"(p0), [p1] "=&v"(p1), [p2] "=&v"(p2), [p3] "=&v"(p3), [cnt] "+s"(cnt), [nap] "+s"(nap)
      : [a] "v"(bell)
      : "vcc", "scc", "memory");
  if (cnt == 0) dead = 1;
}
#define GATHER_M(dl) \
        unsigned oM = slot(t, 0); \
        asm volatile("" : "+s"(oM));        \
        if (wave == 7) wait_bell_bar(slotbase + oM + HINT_OFF, (dl), dead); else lbar(); \
        TRC(0) \
        if (wave < 7) { int sp_ = gather_m(rs, oM, tid, lm, dead); TRV(6, sp_) } \
        TRC(1) \
        lbar();

DI float xg_max_a(float x) {
  float t;
  asm volatile("v_mov_b32 %1, %0\n\ts_nop 1\n\tv_permlane16_swap_b32 %0, %1\n\tv_max_f32 %0, %0, %1\n\tv_mov_b32 %1, %0\n\ts_nop 1\n\tv_permlane32_swap_b32 %0, %1\n\tv_max_f32 %0, %0, %1" : "+v"(x), "=&v"(t));
  return x;
}
DI void ror8_add4(f32x4& v) {
  float a = v[0], b = v[1], c = v[2], d = v[3];
  asm volatile("s_nop 1\n\tv_add_f32_dpp %0, %0, %0 row_ror:8 row_mask:0xf bank_mask:0xf\n\tv_add_f32_dpp %1, %1, %1 row_ror:8 row_mask:0xf bank_mask:0xf\n\t"
               "v_add_f32_dpp %2, %2, %2 row_ror:8 row_mask:0xf bank_mask:0xf\n\tv_add_f32_dpp %3, %3, %3 row_ror:8 row_mask:0xf bank_mask:0xf" : "+v"(a), "+v"(b), "+v"(c), "+v"(d));
  v = f32x4{a, b, c, d};
}
DI void swap32_add4(f32x4& v) {
  float a = v[0], b = v[1], c = v[2], d = v[3], t0, t1, t2, t3;
  asm volatile("v_mov_b32 %4, %0\n\tv_mov_b32 %5, %1\n\tv_mov_b32 %6, %2\n\tv_mov_b32 %7, %3\n\ts_nop 0\n\t"
               "v_permlane32_swap_b32 %0, %4\n\tv_permlane32_swap_b32 %1, %5\n\tv_permlane32_swap_b32 %2, %6\n\tv_permlane32_swap_b32 %3, %7\n\t"
               "v_add_f32 %0, %0, %4\n\tv_add_f32 %1, %1, %5\n\tv_add_f32 %2, %2, %6\n\tv_add_f32 %3, %3, %7"
               : "+v"(a), "+v"(b), "+v"(c), "+v"(d), "=&v"(t0), "=&v"(t1), "=&v"(t2), "=&v"(t3));
  v = f32x4{a, b, c, d};
}

DI float row16_sum(float x) {
  x += __int_as_float(__builtin_amdgcn_update_dpp(0, __float_as_int(x), 0x121, 0xf, 0xf, true));
  x += __int_as_float(__builtin_amdgcn_update_dpp(0, __float_as_int(x), 0x122, 0xf, 0xf, true));
  x += __int_as_float(__builtin_amdgcn_update_dpp(0, __float_as_int(x), 0x124, 0xf, 0xf, true));
  x += __int_as_float(__builtin_amdgcn_update_dpp(0, __float_as_int(x), 0x128, 0xf, 0xf, true));
  return x;
}
template <int AUX>
DI void ln_load(rsrc_t rx, unsigned soff, int tid, u32x4 (&v)[4]) {
  const int row = tid >> 4, i = min(tid & 15, 13);
#pragma unroll
  for (int n = 0; n < 4; ++n) v[n] = __builtin_amdgcn_raw_buffer_load_b128(rx, (unsigned)(row * DM + (4 * i + n) * 8) * 2, soff, AUX);
}
template <class ST>
DI void ln_apply(const u32x4 (&v)[4], int tid, ST store) {
  const int row = tid >> 4, i = tid & 15;
  const bool act = i < 14;
  float s = 0.f;
#pragma unroll
  for (int n = 0; n < 4; ++n) s += (bflo(v[n].x) + bfhi(v[n].x)) + (bflo(v[n].y) + bfhi(v[n].y)) + ((bflo(v[n].z) + bfhi(v[n].z)) + (bflo(v[n].w) + bfhi(v[n].w)));
  const float mu = row16_sum(act ? s : 0.f) * (1.0f / DM);
  float q = 0.f;
#pragma unroll
  for (int n = 0; n < 4; ++n) {
    const unsigned w[4] = {v[n].x, v[n].y, v[n].z, v[n].w};
#pragma unroll
    for (int e = 0; e < 4; ++e) { const float a = bflo(w[e]) - mu, b = bfhi(w[e]) - mu; q += a * a + b * b; }
  }
  const float rs = __builtin_amdgcn_rsqf(row16_sum(act ? q : 0.f) * (1.0f / DM) + 1e-5f);
  if (act) {
#pragma unroll
    for (int n = 0; n < 4; ++n) {
      u32x4 o;
      o.x = cvtpk((bflo(v[n].x) - mu) * rs, (bfhi(v[n].x) - mu) * rs);
      o.y = cvtpk((bflo(v[n].y) - mu) * rs, (bfhi(v[n].y) - mu) * rs);
      o.z = cvtpk((bflo(v[n].z) - mu) * rs, (bfhi(v[n].z) - mu) * rs);
      o.w = cvtpk((bflo(v[n].w) - mu) * rs, (bfhi(v[n].w) - mu) * rs);
      store((4 * i + n) * 32 + row, o);
    }
  }
}
DI float max16(const f32x16& s) {
  float a = __builtin_fmaxf(__builtin_fmaxf(s[0], s[1]), s[2]), b = __builtin_fmaxf(__builtin_fmaxf(s[3], s[4]), s[5]);
  a = __builtin_fmaxf(__builtin_fmaxf(a, s[6]), s[7]); b = __builtin_fmaxf(__builtin_fmaxf(b, s[8]), s[9]);
  a = __builtin_fmaxf(__builtin_fmaxf(a, s[10]), s[11]); b = __builtin_fmaxf(__builtin_fmaxf(b, s[12]), s[13]);
  a = __builtin_fmaxf(__builtin_fmaxf(a, s[14]), s[15]);
  a = __builtin_fmaxf(a, b);
  auto sw = __builtin_amdgcn_permlane32_swap(__float_as_uint(a), __float_as_uint(a), false, false);
  return __builtin_fmaxf(__uint_as_float(sw[0]), __uint_as_float(sw[1]));
}
#define ATT_NAP 40
struct NoMid { DI void operator()() const {} };
template <int NQ, int AUX, bool EARLY = false, class MID = NoMid>
DI void attn_task(char* ws, const char* qkv, int s0, int h, char* smem, int wave, int lane, rsrc_t rb, int& dead, MID mid = MID()) {
  const int r32 = lane & 31, hi = lane >> 5;
  const rsrc_t rk = mkrs(qkv, QKVB);
  const unsigned vk = (unsigned)(wave * 128 + hi * 32 + r32) * 16;
  int rdy = 0;
  auto need = [&](int tl) {
    const int j = 8 * tl + wave;
    if (EARLY && rdy <= j) rdy = 4 * wait_prog(rb, PG_B1K * 4, 4, (j >> 2) + 1, lane, dead, tl < 7 ? ATT_NAP : 2);
  };
  auto ldk = [&](int tl, int st) { return __builtin_amdgcn_raw_buffer_load_b128(rk, vk + st * 1024, (unsigned)(QKV_K + tl * 16384), AUX); };
  auto ldv = [&](int tl, int st) { return __builtin_amdgcn_raw_buffer_load_b128(rk, vk + st * 1024, (unsigned)(QKV_V + tl * 16384), AUX); };
  u32x4 kf[3][2], vf[2][2];
  auto prime = [&]() { need(0); need(1); kf[0][0] = ldk(0, 0); kf[0][1] = ldk(0, 1); kf[1][0] = ldk(1, 0); kf[1][1] = ldk(1, 1); vf[0][0] = ldv(0, 0); vf[0][1] = ldv(0, 1); };
  if (EARLY) {
#pragma unroll
    for (int i = 0; i < NQ; ++i) wait_prog(rb, (PG_B1Q + 2 * ((s0 + i) & 3) + (h >> 3)) * 4, 1, ((s0 + i) >> 2) + 1, lane, dead, ATT_NAP);
    rdy = 4 * wait_prog(rb, PG_B1K * 4, 4, 4, lane, dead, ATT_NAP);
  }
  prime();
  u32x4 q[NQ][2];
  float m[NQ];
  {
    float kn = __uint_as_float(__builtin_amdgcn_raw_buffer_load_b32(rk, (unsigned)(EARLY ? (lane & 15) : lane) * 4, (unsigned)QKV_KN, AUX));
#pragma unroll
    for (int i = 0; i < NQ; ++i) {
      const unsigned vq = (unsigned)((h * SQ + 32 * (s0 + i) + r32) * 4 + hi) * 16;
      q[i][0] = __builtin_amdgcn_raw_buffer_load_b128(rk, vq, (unsigned)QKV_Q, AUX); q[i][1] = __builtin_amdgcn_raw_buffer_load_b128(rk, vq + 32, (unsigned)QKV_Q, AUX);
    }
    kn = fmaxf(kn, __int_as_float(__builtin_amdgcn_update_dpp(0, __float_as_int(kn), 0x121, 0xf, 0xf, true)));
    kn = fmaxf(kn, __int_as_float(__builtin_amdgcn_update_dpp(0, __float_as_int(kn), 0x122, 0xf, 0xf, true)));
    kn = fmaxf(kn, __int_as_float(__builtin_amdgcn_update_dpp(0, __float_as_int(kn), 0x124, 0xf, 0xf, true)));
    kn = fmaxf(kn, __int_as_float(__builtin_amdgcn_update_dpp(0, __float_as_int(kn), 0x128, 0xf, 0xf, true)));
    kn = xg_max(kn);
#pragma unroll
    for (int i = 0; i < NQ; ++i) {
      float qn = 0.f;
      const unsigned w[8] = {q[i][0].x, q[i][0].y, q[i][0].z, q[i][0].w, q[i][1].x, q[i][1].y, q[i][1].z, q[i][1].w};
#pragma unroll
      for (int e = 0; e < 8; ++e) qn += bflo(w[e]) * bflo(w[e]) + bfhi(w[e]) * bfhi(w[e]);
      auto sw = __builtin_amdgcn_permlane32_swap(__float_as_uint(qn), __float_as_uint(qn), false, false);
      qn = __uint_as_float(sw[0]) + __uint_as_float(sw[1]);
      m[i] = __builtin_amdgcn_sqrtf(qn * kn) * 1.001f + 1e-6f;
    }
  }
  float* part = (float*)smem;
  int* redo = (int*)(smem + NQ * 32768 + 64);
  float* mxch = (float*)(smem + NQ * 32768 + 256);
  const f32x16 Z16 = {0, 0, 0, 0, 0, 0, 0, 0, 0, 0, 0, 0, 0, 0, 0, 0};
  for (int pass = 0;; ++pass) {
    f32x16 o[NQ];
#pragma unroll
    for (int i = 0; i < NQ; ++i) {
      o[i] = Z16;
      const unsigned mh = cvtpk(-m[i], 0.f) & 0xffffu;
      const unsigned ml = cvtpk(-m[i] - bflo(mh), 0.f) & 0xffffu;
      if (hi) q[i][1].z = mh | (ml << 16);
    }
#define ATT_P(S, PA, PB) \
      PA.x = cvtpk(ex2(S[0]), ex2(S[1])); PA.y = cvtpk(ex2(S[2]), ex2(S[3])); PA.z = cvtpk(ex2(S[4]), ex2(S[5])); PA.w = cvtpk(ex2(S[6]), ex2(S[7])); \
      PB.x = cvtpk(ex2(S[8]), ex2(S[9])); PB.y = cvtpk(ex2(S[10]), ex2(S[11])); PB.z = cvtpk(ex2(S[12]), ex2(S[13])); PB.w = cvtpk(ex2(S[14]), ex2(S[15]));
    f32x16 cur = mfmaL(kf[0][0], q[0][0], Z16);
    cur = mfmaL(kf[0][1], q[0][1], cur);
#pragma unroll
    for (int tl = 0; tl < 8; ++tl) {
      const int kc = tl % 3, kn1 = (tl + 1) % 3, kn2 = (tl + 2) % 3, vc = tl & 1;
      if (tl + 2 < 8) { need(tl + 2); kf[kn2][0] = ldk(tl + 2, 0); kf[kn2][1] = ldk(tl + 2, 1); }
      if (tl + 1 < 8) { vf[vc ^ 1][0] = ldv(tl + 1, 0); vf[vc ^ 1][1] = ldv(tl + 1, 1); }
#pragma unroll
      for (int i = 0; i < NQ; ++i) {
        f32x16 nxt = cur;
        if (i + 1 < NQ) { nxt = mfmaL(kf[kc][0], q[(i + 1) % NQ][0], Z16); nxt = mfmaL(kf[kc][1], q[(i + 1) % NQ][1], nxt); }
        else if (tl + 1 < 8) { nxt = mfmaL(kf[kn1][0], q[0][0], Z16); nxt = mfmaL(kf[kn1][1], q[0][1], nxt); }
        SB0();
        u32x4 pa, pb;
        ATT_P(cur, pa, pb)
        SB0();
        o[i] = mfmaL(vf[vc][0], pa, o[i]);
        o[i] = mfmaL(vf[vc][1], pb, o[i]);
        cur = nxt;
      }
    }
#undef ATT_P
    mid();
    if (threadIdx.x == 0) redo[0] = 0;
#pragma unroll
    for (int i = 0; i < NQ; ++i)
#pragma unroll
      for (int r = 0; r < 16; r += 4) *(f32x4*)(part + ((wave * NQ + i) * 64 + lane) * 16 + (r ^ ((lane & 3) << 2))) = f32x4{o[i][r], o[i][r + 1], o[i][r + 2], o[i][r + 3]};
    lbar();
    if (wave < 2 * NQ) {
      const int i = wave >> 1, c = wave & 1;
      f32x4 a0 = {0, 0, 0, 0}, a1 = {0, 0, 0, 0};
      float l = 0.f;
      const int ll = 32 + r32;
#pragma unroll
      for (int w = 0; w < 8; ++w) {
        const float* pp = part + ((w * NQ + i) * 64 + lane) * 16;
        a0 += *(const f32x4*)(pp + ((8 * c) ^ ((lane & 3) << 2)));
        a1 += *(const f32x4*)(pp + ((8 * c + 4) ^ ((lane & 3) << 2)));
        l += part[((w * NQ + i) * 64 + ll) * 16 + (12 ^ ((ll & 3) << 2))];
      }
      if (pass == 0 && !(l >= 5.421010862427522e-20f && (!EARLY || l <= 1.099511627776e12f))) redo[0] = 1;
      const float ri = __builtin_amdgcn_rcpf(l);
      u32x4 cv; cv.x = cvtpk(a0[0] * ri, a0[1] * ri); cv.y = cvtpk(a0[2] * ri, a0[3] * ri); cv.z = cvtpk(a1[0] * ri, a1[1] * ri); cv.w = cvtpk(a1[2] * ri, a1[3] * ri);
      const rsrc_t rof = mkrs(ws + WS_OF, OFB);
      __builtin_amdgcn_raw_buffer_store_b128(cv, rof, (unsigned)((((s0 + i) * NH + h) * 4 + 2 * c + hi) * 32 + r32) * 16, 0, AUX);
    }
    lbar();
    if (pass > 0 || !__builtin_amdgcn_readfirstlane(redo[0])) break;
    float mx[NQ];
#pragma unroll
    for (int i = 0; i < NQ; ++i) { mx[i] = -INFINITY; if (hi) q[i][1].z = 0u; }
    for (int tl = 0; tl < 8; ++tl) {
      const u32x4 k0 = ldk(tl, 0), k1 = ldk(tl, 1);
#pragma unroll
      for (int i = 0; i < NQ; ++i) { f32x16 sc = mfmaL(k0, q[i][0], Z16); sc = mfmaL(k1, q[i][1], sc); mx[i] = fmaxf(mx[i], max16(sc)); }
    }
#pragma unroll
    for (int i = 0; i < NQ; ++i) if (hi == 0) mxch[(wave * NQ + i) * 32 + r32] = mx[i];
    lbar();
#pragma unroll
    for (int i = 0; i < NQ; ++i) {
      float v = -INFINITY;
#pragma unroll
      for (int w = 0; w < 8; ++w) v = fmaxf(v, mxch[(w * NQ + i) * 32 + r32]);
      m[i] = v;
    }
    prime();
    lbar();
  }
}

__global__ void __launch_bounds__(512, 2) k_layer(LayerArgs a) {
  extern __shared__ __attribute__((aligned(16))) char smem[];
  u32x4* lm = (u32x4*)smem;
  float* red = (float*)(smem + 8192);
  int* lrole = (int*)(smem + 65536);
  const int tid = threadIdx.x, lane = tid & 63, wave = __builtin_amdgcn_readfirstlane(tid >> 6);
  const int g = lane >> 4, l16 = lane & 15;
  gu32* ctl = (gu32*)(a.ws + WS_CTL);
  unsigned* trc = a.trace;
  (void)trc;
  if (blockIdx.x == 0) SPAN(12)
  gu32* tk = ctl + CTL_ROLE;
  if (tid == 0) {
    const int cls0 = xcc_id() < 4 ? 0 : 1;
    lrole[1] = cls0;
    lrole[2] = (int)__hip_atomic_fetch_add(tk + cls0 * 64, 1u, RLX_AGENT);
  }
  __syncthreads();
  const int cls = __builtin_amdgcn_readfirstlane(lrole[1]);
  const unsigned my = (unsigned)__builtin_amdgcn_readfirstlane(lrole[2]);
  const int tent = (cls == 1 && my < N_GU) ? (int)my : -1;
  auto census = [&]() -> int {
    if (tid == 0) {
      int role = -1;
      if (cls == 0 && my < NCHAIN) role = (int)my;
      else {
        int spins = 0;
        for (;;) {
          const unsigned c0 = __hip_atomic_load(tk, RLX_AGENT), c1 = __hip_atomic_load(tk + 64, RLX_AGENT);
          const bool full = c0 + c1 >= gridDim.x;
          if (cls == 1) {
            if (c0 >= NCHAIN) { if (my < NWORK) role = NCHAIN + (int)my; break; }
            if (full) { const unsigned deficit = NCHAIN - c0; role = my < deficit ? (int)(c0 + my) : (my - deficit < NWORK ? NCHAIN + (int)(my - deficit) : -1); break; }
          } else {
            if (full) { const unsigned w = c1 + (my - NCHAIN); if (w < NWORK) role = NCHAIN + (int)w; break; }
          }
          if (++spins > (1 << 20)) break;
          __builtin_amdgcn_s_sleep(2);
        }
      }
      lrole[0] = role;
    }
    __syncthreads();
    return __builtin_amdgcn_readfirstlane(lrole[0]);
  };
  int dead = 0;
  const f32x4 Z4 = {0, 0, 0, 0};
#define LAYER_VARS(L) \
    const rsrc_t rc = mkrs(a.ws + WS_CTL + CTL_PROG + (size_t)(L) * PROG_LAYER, PROG_LAYER);       \
    const char* slotbase = a.ws + WS_SLOT + (size_t)(L) * SLOT_LAYER; \
    const rsrc_t rs = mkrs(slotbase, SLOT_LAYER); \
    const unsigned long long nearm = __builtin_amdgcn_ballot_w64(ctl[CTL_TBL + (L) * NSEG + lane] != 0u);       \
    auto slot = [&](int t, int i) -> unsigned { return (unsigned)(t * BPS + i) * 16384u + (unsigned)((nearm >> t) & 1ull) * 8192u; }; \
    const u16* xin = (L) ? (const u16*)(a.ws + WS_X2) : a.x0;         \
    u16* x1 = (u16*)(a.ws + WS_X1 + (size_t)(L) * XB);               \
    unsigned* trc = a.trace + (L) * 131072; \
    (void)rs; (void)slot; (void)xin; (void)x1; (void)trc; (void)rc;
  auto gu_role = [&](int w, auto confirm) -> bool {
      if (w == 0) SPAN(13)
      if (w == 55) SPAN(14)
      const int r = w * 8 + wave;
      const bool isG = r < 224;
      const int rr = isG ? r : r - 224, h = rr / 14, u = rr % 14;
      const rsrc_t ro = mkrs(a.ws + (isG ? WS_GF : WS_UF), GFB);
      u32x4* sx = (u32x4*)smem;
      u32x4 pre[4], w0[KS], w1[KS];
      ln_load<16>(mkrs(a.x0, XB), 0, tid, pre);
      const u32x4* W = (const u32x4*)(a.ws + (isG ? WS_AF : WS_BF));
#pragma unroll
      for (int ks = 0; ks < KS; ++ks) {
        w0[ks] = W[(((size_t)h * NT + 2 * u) * KS + ks) * 64 + lane];
        w1[ks] = W[(((size_t)h * NT + 2 * u + 1) * KS + ks) * 64 + lane];
      }
      if (!confirm()) return false;
      SB0();
      for (int L = 0; L < NLAYERS; ++L) {
      LAYER_VARS(L)
      if (L) {
        if (wave == 0) wait_prog(rc, PG_B3 * 4, 4, 1, lane, dead, 4);
        lbar();
        ln_load<16>(mkrs(xin, XB), 0, tid, pre);
      }
      if (w == 0) { asm volatile("" :: "v"(pre[3].x), "v"(pre[0].x)); SPAN(16) }
      ln_apply(pre, tid, [&](int c, u32x4 v) { sx[c] = v; });
      if (w == 0) SPAN(25)
#pragma unroll
      for (int ks = 0; ks < KS; ++ks) asm volatile("" :: "v"(w0[ks]), "v"(w1[ks]));
      if (w == 0) SPAN(15)
      unsigned rdyL = 0;
      auto compute = [&](int s) {
        const u32x4* buf = sx + (s & 1) * 2048;
        if (isG) {
#pragma unroll
          for (int tt = 0; tt < 2; ++tt) {
            f32x4 a0 = Z4, a1 = Z4;
#pragma unroll
            for (int ks = 0; ks < KS; ++ks) {
              const u32x4 x = buf[(4 * ks + g) * 32 + 16 * tt + l16];
              a0 = mfma32(w0[ks], x, a0); a1 = mfma32(w1[ks], x, a1);
            }
            bst4s(pack8(a0, a1), ro, (unsigned)(((h * 2 + tt) * KS + u) * 64 + lane) * 16, (unsigned)s * (NH * 2 * KS * 1024));
          }
        } else {
          f32x4 a00 = Z4, a01 = Z4, a10 = Z4, a11 = Z4;
#pragma unroll
          for (int ks = 0; ks < KS; ++ks) {
            const u32x4 x0 = buf[(4 * ks + g) * 32 + l16], x1 = buf[(4 * ks + g) * 32 + 16 + l16];
            a00 = mfma32(x0, w0[ks], a00); a01 = mfma32(x1, w0[ks], a01);
            a10 = mfma32(x0, w1[ks], a10); a11 = mfma32(x1, w1[ks], a11);
          }
          bst4s(pack8(a00, a01), ro, (unsigned)((h * NT + 2 * u) * 64 + lane) * 16, (unsigned)s * (NH * NT * 1024));
          bst4s(pack8(a10, a11), ro, (unsigned)((h * NT + 2 * u + 1) * 64 + lane) * 16, (unsigned)s * (NH * NT * 1024));
        }
      };
      const unsigned ldsx = (unsigned)(uintptr_t)sx;
      auto fetch = [&](int s) {
#pragma unroll
        for (int n = 0; n < 4; ++n) {
          const int pc = min(wave * 4 + n, 27);
          glds16s(a.ws + WS_XF + (size_t)s * (1792 * 16) + pc * 1024 + lane * 16, (unsigned)__builtin_amdgcn_readfirstlane(ldsx + (s & 1) * 32768 + pc * 1024));
        }
      };
      auto need_ln = [&](int s) {
        const int k = s & 3, need = (s >> 2) + 1;
        if (wave == 0 && (int)((rdyL >> (8 * k)) & 255u) < need) {
          const int r = wait_prog(rc, (PG_LN + k) * 4, 1, need, lane, dead, 1);
          rdyL = (rdyL & ~(255u << (8 * k))) | ((unsigned)min(r, 255) << (8 * k));
        }
      };
      constexpr int GU_EARLY = 5;
      lbar();
      if (w == 0) SPAN(11)
      compute(0);
      drain();
      lbar();
      if (tid == 0) set_prog(rc, (PG_GU + w) * 4, 1u);
      if (w == 0) SPAN(10)
      need_ln(1);
      lbar();
      fetch(1);
      drain();
      for (int s = 1; s < GU_EARLY; ++s) {
        need_ln(s + 1);
        lbar();
        fetch(s + 1);
        compute(s);
        drain();
        lbar();
        if (tid == 0) set_prog(rc, (PG_GU + w) * 4, (unsigned)(s + 1));
        if ((w == 0 || w == 40)) SPAN(1200 + (w ? 16 : 0) + s)
      }
      for (int s = GU_EARLY; s < NSEG; ++s) {
        const int sn = min(s + 1, NSEG - 1);
        need_ln(sn);
        lbar();
        if (tid == 0) set_prog(rc, (PG_GU + w) * 4, (unsigned)max(s - 1, GU_EARLY));
        if ((w == 0 || w == 40) && s < 12) SPAN(1200 + (w ? 16 : 0) + s)
        fetch(sn);
        compute(s);
        asm volatile("s_waitcnt vmcnt(2)" ::: "memory");
      }
      drain();
      lbar();
      if (tid == 0) set_prog(rc, (PG_GU + w) * 4, (unsigned)NSEG);
      }
      return true;
  };
  int role;
  if (tent >= 0) {
    role = -2;
    if (gu_role(tent, [&]() { role = census(); return role == NCHAIN + tent; })) {
      if (dead && lane == 0) ctl[CTL_TMO] = 1;
      return;
    }
  } else role = census();
  for (int i = tid; i < 1024; i += 512) red[i] = 0.f;
  __syncthreads();
  if (blockIdx.x == 255) SPAN(19)
  if (role == NCHAIN - 1) SPAN(20)
  if (role < 0) return;
  unsigned napinfo = 0;
  constexpr int nsteps = NSEG;
  if (role >= NCHAIN) {
    const int w = role - NCHAIN;
    __syncthreads();
    if (w < N_GU) {
      gu_role(w, [&]() { return true; });
    } else if (w < W_XP) {
      if (NLAYERS == 2) {
        const int i = w - W_ATT, grp = i >> 4, h = i & 15;
        const char* qkv = a.ws + WS_QKV;
        const rsrc_t rb = mkrs(a.ws + WS_CTL + CTL_PROG, PROG_LAYER);
        LAYER_VARS(1)
        attn_task<2, 16, true>(a.ws, qkv, 2 * grp, h, smem, wave, lane, rb, dead);
        drain();
        lbar();
        if (tid == 0) set_prog(rc, (PG_ATT + i) * 4, 1u);
        if (i == 0) SPAN(21)
        int cnt = 1;
        for (int s0 = 4 + 2 * grp; s0 < NSEG; s0 += 4) {
          if (wave == 0) {
            wait_prog(rb, (PG_B1Q + 2 * (s0 & 3) + (h >> 3)) * 4, 1, (s0 >> 2) + 1, lane, dead, 8);
            wait_prog(rb, (PG_B1Q + 2 * ((s0 + 1) & 3) + (h >> 3)) * 4, 1, ((s0 + 1) >> 2) + 1, lane, dead, 8);
          }
          lbar();
          attn_task<2, 16>(a.ws, qkv, s0, h, smem, wave, lane, rb, dead);
          drain();
          lbar();
          ++cnt;
          if (tid == 0) set_prog(rc, (PG_ATT + i) * 4, (unsigned)cnt);
          if (i == 0 && cnt == 2) SPAN(22)
        }
      }
    } else if (w >= W_B3 && w < W_B1) {
      if (NLAYERS == 2) {
      LAYER_VARS(1)
      const int i = w - W_B3, par = i >> 2, cg = i & 3;
      const int ct = min(cg * 7 + wave, NT - 1);
      const rsrc_t rof = mkrs(a.ws + WS_OF, OFB), rx2 = mkrs(xin, XB), rxr = mkrs(a.ws + WS_X1, XB);
      const u32x4* W = (const u32x4*)(a.ws + WS_WOF) + (size_t)ct * NH * 64 + lane;
      u32x4 wo[NH];
#pragma unroll
      for (int h = 0; h < NH; ++h) wo[h] = W[h * 64];
      u32x4* so = (u32x4*)smem;
      int cnt = 0;
      for (int s = par; s < NSEG; s += 2) {
        if (wave == 0) wait_prog(rc, (PG_ATT + ((s >> 1) & 1) * 16) * 4, 16, (s >> 2) + 1, lane, dead, s < 8 ? 1 : 4);
        lbar();
        u32x2 xv[2];
#pragma unroll
        for (int tt = 0; tt < 2; ++tt) xv[tt] = __builtin_amdgcn_raw_buffer_load_b64(rxr, (unsigned)((32 * s + 16 * tt + l16) * DM + 16 * ct + 4 * g) * 2, 0, 16);
        u32x4 ov[4];
#pragma unroll
        for (int n = 0; n < 4; ++n) ov[n] = bld4s(rof, (unsigned)(tid + 512 * n) * 16, (unsigned)s * (NH * 2048));
#pragma unroll
        for (int n = 0; n < 4; ++n) so[tid + 512 * n] = ov[n];
        lbar();
        f32x4 acc[2] = {Z4, Z4};
#pragma unroll
        for (int h = 0; h < NH; ++h)
#pragma unroll
          for (int tt = 0; tt < 2; ++tt) acc[tt] = mfma32(wo[h], so[h * 128 + g * 32 + 16 * tt + l16], acc[tt]);
#pragma unroll
        for (int tt = 0; tt < 2; ++tt) {
          const unsigned c0 = cvtpk(bflo(xv[tt].x) + acc[tt][0], bfhi(xv[tt].x) + acc[tt][1]), c1 = cvtpk(bflo(xv[tt].y) + acc[tt][2], bfhi(xv[tt].y) + acc[tt][3]);
          const unsigned n0 = __builtin_amdgcn_permlane16_swap(c0, c0, false, false)[1], n1 = __builtin_amdgcn_permlane16_swap(c1, c1, false, false)[1];
          if (wave < 7 && (g & 1) == 0) bst4s(u32x4{c0, c1, n0, n1}, rx2, (unsigned)((32 * s + 16 * tt + l16) * DM + 16 * ct + 4 * g) * 2, 0);
        }
        drain();
        lbar();
        ++cnt;
        if (tid == 0) set_prog(rc, (PG_B3 + i) * 4, (unsigned)cnt);
        if (i == 0 && cnt <= 2) SPAN(22 + cnt)
      }
      }
    } else if (w < W_XO) {
      const int i = w - W_XP, sg = i >> 2, hg = i & 3;
      const int par = wave & 1, qt = (wave >> 1) & 1, p = 2 * hg + (wave >> 2), h = 2 * p + par;
      const rsrc_t rxf = mkrs(a.ws + WS_XF, XFB);
      const rsrc_t rg = mkrs(a.ws + WS_GF, GFB);
      const int mkey = 2 * (l16 >> 2) + (l16 & 1);
      for (int L = 0; L < NLAYERS; ++L) {
      LAYER_VARS(L)
      const rsrc_t rgm = mkrs(a.ws + WS_GMF + L * GMFB, GMFB), rp = mkrs(a.ws + WS_PF + L * PFB, PFB);
      int rdyG = 0, cnt = 0;
      for (int s = sg; s < NSEG; s += 4) {
        if (wave == 0) {
          wait_prog(rc, (PG_LN + (s & 3)) * 4, 1, (s >> 2) + 1, lane, dead, 8);
          if (rdyG <= s) rdyG = wait_prog(rc, PG_GU * 4, 28, s + 1, lane, dead, 8);
        }
        lbar();
        u32x4 x[KS];
#pragma unroll
        for (int ks = 0; ks < KS; ++ks) x[ks] = bld4s(rxf, (unsigned)((4 * ks + g) * 32 + 16 * qt + l16) * 16, (unsigned)s * (1792 * 16));
        f32x4 sm = Z4, s0 = Z4, s1 = Z4;
        const unsigned sgo = (unsigned)(s * NH + h) * (2 * KS * 1024);
        u32x4 gf[KS];
#pragma unroll
        for (int ks = 0; ks < KS; ++ks) gf[ks] = bld4s(rg, (unsigned)(ks * 64 + lane) * 16, sgo);
        SB0();
#pragma unroll
        for (int ks = 0; ks < KS; ++ks) s0 = mfma32(gf[ks], x[ks], s0);
        if (qt) {
#pragma unroll
          for (int ks = 0; ks < KS; ++ks) gf[ks] = bld4s(rg, (unsigned)((KS + ks) * 64 + lane) * 16, sgo);
          SB0();
#pragma unroll
          for (int ks = 0; ks < KS; ++ks) s1 = mfma32(gf[ks], x[ks], s1);
        }
        if (wave == 0) wait_prog(rc, (PG_SP + hg * 16) * 4, 16, s + 1, lane, dead, s >= 60 ? 0 : 4);
        lbar();
        const unsigned smo = (unsigned)(s * NH + h) * 7168;
#pragma unroll
        for (int ks = 0; ks < KS; ++ks) gf[ks] = bld4s(rgm, (unsigned)(ks * 32 + g * 8 + mkey) * 16, smo);
        SB0();
#pragma unroll
        for (int ks = 0; ks < KS; ++ks) sm = mfma32(gf[ks], x[ks], sm);
        const float NEG = -INFINITY;
#pragma unroll
        for (int r = 0; r < 4; ++r) {
          const int key = 4 * g + r;
          if (qt == 0) { if (key > l16) s0[r] = NEG; s1[r] = NEG; }
          else { if (key > l16) s1[r] = NEG; }
        }
        float m = fmaxf(fmaxf(fmaxf(s0[0], s0[1]), fmaxf(s0[2], s0[3])), fmaxf(fmaxf(s1[0], s1[1]), fmaxf(s1[2], s1[3])));
        m = xg_max(fmaxf(m, fmaxf(sm[0], sm[1])));
        f32x4 e0, e1;
#pragma unroll
        for (int r = 0; r < 4; ++r) { e0[r] = ex2(s0[r] - m); e1[r] = ex2(s1[r] - m); }
        const float em0 = ex2(sm[0] - m), em1 = ex2(sm[1] - m);
        float l = (e0[0] + e0[1]) + (e0[2] + e0[3]) + ((e1[0] + e1[1]) + (e1[2] + e1[3])) + (em0 + em1);
        l = xg_sum(l);
        const float ri = __builtin_amdgcn_rcpf(l);
        const unsigned spo = (unsigned)((s * 8 + p) * 2 + qt) * 2560;
        bst4s(pack8(e0 * ri, e1 * ri), rp, (unsigned)(par * 1024 + lane * 16), spo);
        __builtin_amdgcn_raw_buffer_store_b32(cvtpk(em0 * ri, em1 * ri), rp, (unsigned)(2048 + par * 256 + lane * 4), spo, 16);
        drain();
        lbar();
        ++cnt;
        if (tid == 0) set_prog(rc, (PG_XP + i) * 4, (unsigned)cnt);
        if (s == NSEG - 1 && hg == 0) SPAN(8)
      }
      }
    } else if (w < W_B3) {
      const int i = w - W_XO, sg = i >> 2, cg = i & 3;
      const int ct = min(cg * 7 + wave, NT - 1);
      const bool act = wave < 7;
      const rsrc_t ru = mkrs(a.ws + WS_UF, GFB);
      u32x4* sp = (u32x4*)smem;
      for (int L = 0; L < NLAYERS; ++L) {
      LAYER_VARS(L)
      const rsrc_t rt2 = mkrs(a.ws + WS_T2F + L * GMFB, GMFB), rp = mkrs(a.ws + WS_PF + L * PFB, PFB), rx1 = mkrs(x1, XB), rxin = mkrs(xin, XB);
      int rdyU = 0, cnt = 0;
      for (int s = sg; s < NSEG; s += 4) {
        if (wave == 0 && rdyU <= s) rdyU = wait_prog(rc, (PG_GU + 28) * 4, 28, s + 1, lane, dead, 8);
        lbar();
        u32x4 uf[NH];
#pragma unroll
        for (int h = 0; h < NH; ++h) uf[h] = bld4s(ru, (unsigned)((h * NT + ct) * 64 + lane) * 16, (unsigned)s * (NH * NT * 1024));
        u32x2 xv[2];
#pragma unroll
        for (int qt = 0; qt < 2; ++qt) xv[qt] = __builtin_amdgcn_raw_buffer_load_b64(rxin, (unsigned)((32 * s + 16 * qt + l16) * DM + 16 * ct + 4 * g) * 2, 0, 16);
        if (wave == 0) {
          wait_prog(rc, (PG_O + cg * 7) * 4, 7, s + 1, lane, dead, 4);
          wait_prog(rc, (PG_XP + (s & 3) * 4) * 4, 4, (s >> 2) + 1, lane, dead, s >= 60 ? 0 : 2);
        }
        lbar();
        u32x2 t2[8];
#pragma unroll
        for (int p = 0; p < 8; ++p) t2[p] = __builtin_amdgcn_raw_buffer_load_b64(rt2, (unsigned)((p * NT + ct) * 64 + lane) * 8, (unsigned)s * (8 * NT * 512), 16);
        {
          u32x4 pv[5];
#pragma unroll
          for (int n = 0; n < 5; ++n) pv[n] = bld4s(rp, (unsigned)(tid + 512 * n) * 16, (unsigned)s * (8 * 2 * 2560));
#pragma unroll
          for (int n = 0; n < 5; ++n) sp[tid + 512 * n] = pv[n];
        }
        lbar();
        f32x4 acc[2] = {Z4, Z4}, accm[2] = {Z4, Z4};
#pragma unroll
        for (int p = 0; p < 8; ++p)
#pragma unroll
          for (int qt = 0; qt < 2; ++qt) {
            const u32x4* P = sp + (p * 2 + qt) * 160;
            acc[qt] = mfma32(uf[2 * p], P[lane], acc[qt]);
            acc[qt] = mfma32(uf[2 * p + 1], P[64 + lane], acc[qt]);
            const unsigned* Pm = (const unsigned*)(P + 128);
            accm[qt] = mfma16(t2[p], u32x2{Pm[lane], Pm[64 + lane]}, accm[qt]);
          }
#pragma unroll
        for (int qt = 0; qt < 2; ++qt) {
          const f32x4 o = acc[qt] + accm[qt];
          const unsigned c0 = cvtpk(bflo(xv[qt].x) + o[0], bfhi(xv[qt].x) + o[1]), c1 = cvtpk(bflo(xv[qt].y) + o[2], bfhi(xv[qt].y) + o[3]);
          const unsigned n0 = __builtin_amdgcn_permlane16_swap(c0, c0, false, false)[1], n1 = __builtin_amdgcn_permlane16_swap(c1, c1, false, false)[1];
          if (act && (g & 1) == 0) bst4s(u32x4{c0, c1, n0, n1}, rx1, (unsigned)((32 * s + 16 * qt + l16) * DM + 16 * ct + 4 * g) * 2, 0);
        }
        drain();
        lbar();
        ++cnt;
        if (tid == 0) set_prog(rc, (PG_XO + i) * 4, (unsigned)cnt);
        if (s == NSEG - 1 && cg == 0) SPAN(9)
      }
      }
    } else if (w >= W_LN) {
      if (w == W_LN) SPAN(17)
      const int i = w - W_LN;
      for (int L = 0; L < NLAYERS; ++L) {
      LAYER_VARS(L)
      const rsrc_t rx = mkrs(xin, XB), rxf = mkrs(a.ws + WS_XF, XFB);
      int cnt = 0;
      for (int s = i; s < NSEG; s += 4) {
        if (L) { if (wave == 0) wait_prog(rc, (PG_B3 + (s & 1) * 4) * 4, 4, (s >> 1) + 1, lane, dead, s < 8 ? 0 : 2); lbar(); }
        u32x4 v4[4];
        ln_load<16>(rx, (unsigned)s * (32 * DM * 2), tid, v4);
        ln_apply(v4, tid, [&](int c, u32x4 v) { bst4s(v, rxf, (unsigned)c * 16, (unsigned)s * (1792 * 16)); });
        drain();
        lbar();
        ++cnt;
        if (tid == 0) set_prog(rc, (PG_LN + i) * 4, (unsigned)cnt);
        if (s == 0) SPAN(18)
      }
      }
    } else {
      const int b = (w - W_B1) % 3, sg = (w - W_B1) / 3;
      u32x4* sx = (u32x4*)smem;
      const int kind = b < 2 ? 0 : (wave == 0 ? 1 : (wave == 1 ? 2 : 3));
      const int h = b * 8 + wave;
      const u32x4* W = (const u32x4*)(a.ws + (kind == 0 ? WS_WQF + (size_t)(h & 15) * 2 * KS * 1024 : (kind == 2 ? WS_WVF : WS_WKF))) + lane;
      u32x4 w0[KS], w1[KS];
#pragma unroll
      for (int ks = 0; ks < KS; ++ks) { w0[ks] = W[ks * 64]; w1[ks] = W[(KS + ks) * 64]; }
      for (int L = 0; L < NLAYERS; ++L) {
      LAYER_VARS(L)
      const rsrc_t rx1 = mkrs(x1, XB), rq = mkrs(a.ws + WS_QKV + (size_t)L * QKVB, QKVB);
      int cnt = 0;
      for (int s = sg; s < NSEG; s += 4) {
        if (wave == 0) wait_prog(rc, (PG_XO + (s & 3) * 4) * 4, 4, (s >> 2) + 1, lane, dead, s >= 60 ? 0 : 4);
        lbar();
        u32x4 v4[4];
        ln_load<16>(rx1, (unsigned)s * (32 * DM * 2), tid, v4);
        ln_apply(v4, tid, [&](int c, u32x4 v) { sx[c] = v; });
        lbar();
        if (kind <= 1) {
          f32x4 ac[2][2] = {{Z4, Z4}, {Z4, Z4}};
#pragma unroll
          for (int ks = 0; ks < KS; ++ks)
#pragma unroll
            for (int tt = 0; tt < 2; ++tt) {
              const u32x4 x = sx[(4 * ks + g) * 32 + 16 * tt + l16];
              ac[tt][0] = mfma32(w0[ks], x, ac[tt][0]); ac[tt][1] = mfma32(w1[ks], x, ac[tt][1]);
            }
          if (kind == 0) {
#pragma unroll
            for (int tt = 0; tt < 2; ++tt) bst4s(pack8(ac[tt][0] * B_SCALE, ac[tt][1] * B_SCALE), rq, (unsigned)((h * SQ + 32 * s + 16 * tt + l16) * 4 + g) * 16, (unsigned)QKV_Q);
          } else {
            float kn = 0.f;
#pragma unroll
            for (int tt = 0; tt < 2; ++tt) {
              u32x4 kv = pack8(ac[tt][0], ac[tt][1]);
              { u32x4 ks = kv; if (g == 3) ks.z = 0x3F803F80u; bst4s(ks, rq, (unsigned)((s * 4 + g) * 32 + 16 * tt + l16) * 16, (unsigned)QKV_K); }
              const float n2 = xg_sum(bflo(kv.x) * bflo(kv.x) + bfhi(kv.x) * bfhi(kv.x) + bflo(kv.y) * bflo(kv.y) + bfhi(kv.y) * bfhi(kv.y) +
                                      bflo(kv.z) * bflo(kv.z) + bfhi(kv.z) * bfhi(kv.z) + bflo(kv.w) * bflo(kv.w) + bfhi(kv.w) * bfhi(kv.w));
              kn = fmaxf(kn, n2);
            }
#pragma unroll
            for (int d = 1; d < 16; d <<= 1) kn = fmaxf(kn, __shfl_xor(kn, d));
            if (lane == 0) __builtin_amdgcn_raw_buffer_store_b32(__float_as_uint(kn), rq, (unsigned)s * 4, (unsigned)QKV_KN, 16);
          }
        } else if (kind == 2) {
          f32x4 ac[2][2] = {{Z4, Z4}, {Z4, Z4}};
#pragma unroll
          for (int ks = 0; ks < KS; ++ks)
#pragma unroll
            for (int p = 0; p < 2; ++p) {
              const int tok = 16 * (l16 >> 3) + 8 * p + 4 * ((l16 >> 2) & 1) + (l16 & 3);
              const u32x4 x = sx[(4 * ks + g) * 32 + tok];
              ac[0][p] = mfma32(x, w0[ks], ac[0][p]); ac[1][p] = mfma32(x, w1[ks], ac[1][p]);
            }
#pragma unroll
          for (int dt = 0; dt < 2; ++dt) {
            const int d = 16 * dt + l16;
            u32x4 v = pack8(ac[dt][0], ac[dt][1]);
            if (d == 28) v = u32x4{0x3F803F80u, 0x3F803F80u, 0x3F803F80u, 0x3F803F80u};
            bst4s(v, rq, (unsigned)(((s * 2 + (g >> 1)) * 2 + (g & 1)) * 32 + d) * 16, (unsigned)QKV_V);
          }
        }
        drain();
        lbar();
        ++cnt;
        if (tid == 0) set_prog(rc, (b == 2 ? PG_B1K + sg : PG_B1Q + 2 * sg + b) * 4, (unsigned)cnt);
      }
      }
    }
  } else if (role >= N_O + N_SP) {
    const int slM = a.sleepX;
    const int h = role - N_O - N_SP;
    const unsigned hoff = (unsigned)(h % 6) * HEAD_BLK;
    const int blk = 1 + h / 6;
    const int mrd = g * 8 + (l16 & 7);
    if (wave == 0) {
      const rsrc_t rg = mkrs(a.ws + WS_GF, GFB);
      const unsigned vg = (unsigned)lane * 16, sg0 = (unsigned)h * 2 * KS * 1024;
      for (int L = 0; L < NLAYERS; ++L) {
      LAYER_VARS(L)
      int rdy = wait_prog(rc, PG_GU * 4, 28, 1, lane, dead, L ? 4 : 1);
      u32x4 w0[KS], w1[KS];
#pragma unroll
      for (int ks = 0; ks < KS; ++ks) { w0[ks] = bld4s(rg, vg + ks * 1024, sg0); w1[ks] = bld4s(rg, vg + (KS + ks) * 1024, sg0); }
      const unsigned vo = hoff + 512 + (unsigned)(g * 8 + l16) * 16, voL = hoff + 1024 + (unsigned)l16 * 4;
      TRDECL
      for (int t = 0; t < nsteps - 1; ++t) {
        unsigned oS = slot(t, blk);
        asm volatile("" : "+s"(oS));
        GATHER_M(slM)
        const bool refresh = rdy <= t + 4 && rdy < NSEG;
        unsigned pgv = 0xFFFFFFFFu;
        if (refresh) pgv = prog_request(rc, PG_GU * 4, 28, lane);
        u32x4 mf[KS];
#pragma unroll
        for (int ks = 0; ks < KS; ++ks) mf[ks] = lm[ks * 32 + mrd];
        SB0();
        f32x4 a0 = Z4, a1 = Z4;
#pragma unroll
        for (int ks = 0; ks < KS; ++ks) { a0 = mfma32(w0[ks], mf[ks], a0); a1 = mfma32(w1[ks], mf[ks], a1); }
        float m = __builtin_fmaxf(__builtin_fmaxf(__builtin_fmaxf(a0[0], a0[1]), a0[2]), __builtin_fmaxf(__builtin_fmaxf(a0[3], a1[0]), a1[1]));
        m = xg_max_a(__builtin_fmaxf(__builtin_fmaxf(m, a1[2]), a1[3]));
        u32x4 pv;
        pv.x = cvtpk(ex2(a0[0] - m), ex2(a0[1] - m)); pv.y = cvtpk(ex2(a0[2] - m), ex2(a0[3] - m));
        pv.z = cvtpk(ex2(a1[0] - m), ex2(a1[1] - m)); pv.w = cvtpk(ex2(a1[2] - m), ex2(a1[3] - m));
        if (l16 < 8) {
          bst4s(pv, rs, vo, oS);
          if (g == 0) __builtin_amdgcn_raw_buffer_store_b32(__float_as_uint(m), rs, voL, oS, 16);
        }
        TRC(3)
        lbar();
        SB0();
        const int tn = min(t + 1, nsteps - 2);
        if (refresh) rdy = prog_min(pgv, rdy);
        if (rdy <= tn) rdy = wait_prog(rc, PG_GU * 4, 28, tn + 1, lane, dead, 1);
        const unsigned sg = sg0 + (unsigned)tn * (NH * 2 * KS * 1024);
#pragma unroll
        for (int ks = 0; ks < KS; ++ks) { w0[ks] = bld4s(rg, vg + ks * 1024, sg); w1[ks] = bld4s(rg, vg + (KS + ks) * 1024, sg); }
        TRFLUSH
      }
      }
    } else {
      for (int L = 0; L < NLAYERS; ++L) {
      LAYER_VARS(L)
      TRDECL
      for (int t = 0; t < nsteps - 1; ++t) {
        GATHER_M(slM)
        lbar();
        TRFLUSH
      }
      }
    }
    napinfo = 0;
  } else if (role >= N_O) {
    const int slM = a.sleepS;
    const int h = (role - N_O) >> 2, j = (role - N_O) & 3;
    const int u = (j == 0) ? wave : (j == 1 ? 4 + wave : (j == 2 ? 8 + wave : 11 + wave));
    const bool pair = wave < (j < 2 ? 4 : 3);
    const unsigned hoff = (unsigned)(h % 6) * HEAD_BLK;
    const int blk = 1 + h / 6;
    const int mrd = g * 8 + (l16 & 7);
    if (pair) {
      const u32x4* AF = (const u32x4*)(a.ws + WS_AF);
      const unsigned vgm = (unsigned)(((h * KS + u) * 32 + g * 8 + l16) * 16);
      u32x4 w0[KS], w1[KS];
#pragma unroll
      for (int ks = 0; ks < KS; ++ks) {
        w0[ks] = AF[(((size_t)h * NT + 2 * u) * KS + ks) * 64 + lane];
        w1[ks] = AF[(((size_t)h * NT + 2 * u + 1) * KS + ks) * 64 + lane];
      }
      for (int L = 0; L < NLAYERS; ++L) {
      LAYER_VARS(L)
      const rsrc_t rgm = mkrs(a.ws + WS_GMF + L * GMFB, GMFB);
      TRDECL
      for (int t = 0; t < nsteps; ++t) {
        GATHER_M(slM)
        u32x4 mf[KS];
#pragma unroll
        for (int ks = 0; ks < KS; ++ks) mf[ks] = lm[ks * 32 + mrd];
        const u32x4 mu = lm[u * 32 + mrd];
        SB0();
        f32x4 a0 = Z4, a1 = Z4;
#pragma unroll
        for (int ks = 0; ks < KS; ++ks) { a0 = mfma32(w0[ks], mf[ks], a0); a1 = mfma32(w1[ks], mf[ks], a1); }
        const u32x4 gm = pack8(a0, a1);
        const f32x4 d = mfma32(gm, mu, Z4);
        if (l16 < 8 && g < 2) *(f32x4*)(red + wave * 64 + l16 * 8 + 4 * g) = d;
        lbar();
        lbar();
        if (l16 < 8) bst4s(gm, rgm, vgm, (unsigned)t * (NH * 7168));
        TRFLUSH
      }
      drain();
      lbar();
      }
    } else {
      for (int L = 0; L < NLAYERS; ++L) {
      LAYER_VARS(L)
      TRDECL
      for (int t = 0; t < nsteps; ++t) {
        unsigned oS = slot(t, blk);
        asm volatile("" : "+s"(oS));
        GATHER_M(slM)
        if (wave == 5 && lane == 0 && t > 0) set_prog(rc, (PG_SP + role - N_O) * 4, (unsigned)t);
        lbar();
        if (wave == 4 && t < nsteps - 1) {
          if (lane < 16) {
            const f32x4 s = (*(const f32x4*)(red + lane * 4) + *(const f32x4*)(red + 64 + lane * 4)) + (*(const f32x4*)(red + 128 + lane * 4) + *(const f32x4*)(red + 192 + lane * 4));
            const unsigned c0 = cvtpk(s[0], s[1]), c1 = cvtpk(s[2], s[3]);
            const unsigned n0 = __builtin_amdgcn_update_dpp(0, c0, 0xB1, 0xf, 0xf, true), n1 = __builtin_amdgcn_update_dpp(0, c1, 0xB1, 0xf, 0xf, true);
            if ((lane & 1) == 0) bst4s(u32x4{c0, c1, n0, n1}, rs, hoff + j * 128 + lane * 8, oS);
          }
          TRC(2)
        }
        lbar();
        TRFLUSH
      }
      lbar();
      if (wave == 5 && lane == 0) set_prog(rc, (PG_SP + role - N_O) * 4, (unsigned)nsteps);
      }
    }
    napinfo = 0;
  } else {
    const int slM = a.sleepO, slP = a.sleepP;
    const int ct = role;
    if (ct == 0) SPAN(5)
    const int par = (lane >> 3) & 1, q = lane & 7;
    const int ha = 2 * wave, hb = 2 * wave + 1;
    const u32x4* BF = (const u32x4*)(a.ws + WS_BF);
    const rsrc_t ru = mkrs(a.ws + WS_UF, GFB);
    const unsigned vua = (unsigned)((ha * NT + ct) * 64 + lane) * 16, vub = vua + NT * 1024;
    const unsigned vt2 = (unsigned)((wave * NT + ct) * 64 + lane) * 8;
    u32x4 wa[KS], wb[KS];
#pragma unroll
    for (int ks = 0; ks < KS; ++ks) {
      wa[ks] = BF[(((size_t)ha * NT + ct) * KS + ks) * 64 + lane];
      wb[ks] = BF[(((size_t)hb * NT + ct) * KS + ks) * 64 + lane];
    }
    if (ct == 0) { asm volatile("" :: "v"(wa[13].x), "v"(wb[13].x)); SPAN(6) }
    const u32x4 ones8 = {0x3F803F80u, 0x3F803F80u, 0x3F803F80u, 0x3F803F80u};
    const u32x2 ones4 = {0x3F803F80u, 0x3F803F80u};
    const int mrd = g * 8 + 2 * (l16 >> 2) + (l16 & 1);
    const unsigned hoff = (unsigned)((2 * wave) % 6 + par) * HEAD_BLK;
    const int blk = 1 + wave / 3;
    const unsigned voP = hoff + (unsigned)(g * 128 + q * 16), voE = hoff + 512 + (unsigned)(g * 8 + q) * 16, voL = hoff + 1024 + (unsigned)q * 4;
    u32x4 asel;
    { const int key = 2 * (l16 >> 2) + (l16 & 1); const unsigned one = (key & 1) ? 0x3F800000u : 0x00003F80u;
      asel.x = (key >> 1) == 0 ? one : 0u; asel.y = (key >> 1) == 1 ? one : 0u; asel.z = (key >> 1) == 2 ? one : 0u; asel.w = (key >> 1) == 3 ? one : 0u; }
#define T2_STAGE() \
      u32x4 mf[KS]; \
      _Pragma("unroll") for (int ks = 0; ks < KS; ++ks) mf[ks] = lm[ks * 32 + mrd]; \
      SB0(); \
      f32x4 aA = Z4, aB = Z4; \
      _Pragma("unroll") for (int ks = 0; ks < KS; ++ks) { aA = mfma32(mf[ks], wa[ks], aA); aB = mfma32(mf[ks], wb[ks], aB); } \
      u32x2 t2; t2.x = cvtpk(aA[0], aA[1]); t2.y = cvtpk(aB[0], aB[1]);
    for (int L = 0; L < NLAYERS; ++L) {
    LAYER_VARS(L)
    const rsrc_t rt2 = mkrs(a.ws + WS_T2F + L * GMFB, GMFB);
    int rdy = wait_prog(rc, (PG_GU + 28) * 4, 28, 1, lane, dead, L ? 4 : 1);
    if (ct == 0) SPAN(7)
    u32x2 t2p = {0u, 0u};
    TRDECL
    for (int t = 0; t < nsteps - 1; ++t) {
      if (ct == 0 && (t == 2 || t == 62)) SPAN(t == 2 ? 0 : 1)
      if (ct == 0 && t == 0) SPAN(2)
      if (ct == 1) SPAN(1000 + t + 24)
      unsigned oS = slot(t, blk), oMn = slot(t + 1, 0);
      asm volatile("" : "+s"(oS), "+s"(oMn));
      GATHER_M(slM)
      if (t > 0) __builtin_amdgcn_raw_buffer_store_b64(t2p, rt2, vt2, (unsigned)(t - 1) * (8 * NT * 512), 16);
      if (wave == 7 && lane == 0 && t > 1) set_prog(rc, (PG_O + ct) * 4, (unsigned)(t - 1));
      if (rdy <= t) rdy = wait_prog(rc, (PG_GU + 28) * 4, 28, t + 1, lane, dead, 4);
      const u32x4 ua = bld4s(ru, vua, (unsigned)t * (NH * NT * 1024)), ub = bld4s(ru, vub, (unsigned)t * (NH * NT * 1024));
      const bool refresh = rdy <= t + 3 && rdy < NSEG;
      unsigned pv = 0xFFFFFFFFu;
      if (refresh) pv = prog_request(rc, (PG_GU + 28) * 4, 28, lane);
      SB0();
      T2_STAGE()
      lbar();
      snooze(slP);
      TRC(2)
      u32x4 P, E; unsigned Lb;
      {
        int spins = 0;
        for (;;) {
          P = bld4s(rs, voP, oS);
          E = bld4s(rs, voE, oS);
          Lb = __builtin_amdgcn_raw_buffer_load_b32(rs, voL, oS, 16);
          unsigned mm = max(max(P.x, P.y), max(P.z, P.w));
          mm = max(mm, max(max(max(E.x, E.y), max(E.z, E.w)), Lb));
          if (__builtin_amdgcn_ballot_w64(mm == SENT) == 0) break;
          if (dead || ++spins > SPINCAP) { dead = 1; break; }
          asm volatile("" ::: "memory");
        }
        TRV(5, spins)
      }
      if (ct == 0 && wave == 7 && lane == 0) __builtin_amdgcn_raw_buffer_store_b32(1u, rs, HINT_OFF, oMn, 16);
      TRC(3)
      const f32x4 Sm = mfma32(asel, P, Z4);
      const f32x4 Da = mfma32(ua, E, Z4);
      const f32x4 Db = mfma32(ub, E, Z4);
      const f32x4 Dx = mfma32(ones8, E, Z4);
      const float mx = __uint_as_float(Lb);
      const float d0 = Sm[0] - mx, d1 = Sm[1] - mx;
      float e0 = ex2(d0), e1 = ex2(d1), lx = Dx[0];
      f32x4 dx;
#pragma unroll
      for (int r = 0; r < 4; ++r) dx[r] = par ? Db[r] : Da[r];
      if (__builtin_amdgcn_ballot_w64(!(d0 <= 30.f && d1 <= 30.f)) != 0) {
        const float dm = fmaxf(xg_max(fmaxf(d0, d1)), 0.f), wx = ex2(-dm);
        e0 = ex2(d0 - dm); e1 = ex2(d1 - dm); lx *= wx; dx *= wx;
      }
      const unsigned pk = cvtpk(e0, e1);
      u32x2 pm; pm.x = par ? 0u : pk; pm.y = par ? pk : 0u;
      const f32x4 Dm = mfma16(t2, pm, Z4);
      const f32x4 Dl = mfma16(ones4, pm, Z4);
      const float ri = __builtin_amdgcn_rcpf(Dl[0] + lx);
      f32x4 o = (Dm + dx) * ri;
      ror8_add4(o);
      if (par == 0) *(f32x4*)(red + wave * 128 + q * 16 + 4 * g) = o;
      lbar();
      if (wave == 0) {
        const int hf = lane >> 5, qq = (lane >> 2) & 7, g4 = lane & 3;
        const float* rp = red + hf * 512 + qq * 16 + 4 * g4;
        f32x4 s = (*(const f32x4*)(rp) + *(const f32x4*)(rp + 128)) + (*(const f32x4*)(rp + 256) + *(const f32x4*)(rp + 384));
        swap32_add4(s);
        const unsigned c0 = cvtpk(s[0], s[1]), c1 = cvtpk(s[2], s[3]);
        const unsigned n0 = __builtin_amdgcn_update_dpp(0, c0, 0xB1, 0xf, 0xf, true);
        const unsigned n1 = __builtin_amdgcn_update_dpp(0, c1, 0xB1, 0xf, 0xf, true);
        if (lane < 32 && (g4 & 1) == 0) bst4s(u32x4{c0, c1, n0, n1}, rs, (unsigned)ct * 256 + (unsigned)((g4 >> 1) * 8 + qq) * 16, oMn);
        TRC(4)
      }
      t2p = t2;
      if (refresh) rdy = prog_min(pv, rdy);
      TRFLUSH
    }
    {
      const int t = nsteps - 1;
      GATHER_M(slM)
      __builtin_amdgcn_raw_buffer_store_b64(t2p, rt2, vt2, (unsigned)(t - 1) * (8 * NT * 512), 16);
      T2_STAGE()
      __builtin_amdgcn_raw_buffer_store_b64(t2, rt2, vt2, (unsigned)t * (8 * NT * 512), 16);
      drain();
      lbar();
      if (wave == 7 && lane == 0) set_prog(rc, (PG_O + ct) * 4, (unsigned)nsteps);
      if (ct == 0) SPAN(3)
      TRFLUSH
    }
    }
    napinfo = 0;
  }
  if (dead && lane == 0) ctl[CTL_TMO] = 1;
}

__global__ void __launch_bounds__(512, 2) k_fin(char* ws, int set, const u16* xin, u16* xout) {
  extern __shared__ __attribute__((aligned(16))) char smem[];
  const int tid = threadIdx.x, lane = tid & 63, wave = __builtin_amdgcn_readfirstlane(tid >> 6);
  const int g = lane >> 4, l16 = lane & 15;
#define FSP(i) {}
  FSP(0)
  const int s = blockIdx.x >> 2, ct = min((blockIdx.x & 3) * 7 + wave, NT - 1);
  const u32x4* W = (const u32x4*)(ws + WS_WOF) + (size_t)ct * NH * 64 + lane;
  u32x4 w[NH];
  u32x2 xv[2];
  int dead = 0;
  const rsrc_t rc = mkrs(ws + WS_CTL + CTL_FIN, 256 * 128);
  attn_task<4, 16>(ws, ws + WS_QKV + (size_t)set * QKVB, 4 * (blockIdx.x >> 4), blockIdx.x & 15, smem, wave, lane, rc, dead, [&]() {
#pragma unroll
    for (int h = 0; h < NH / 2; ++h) w[h] = W[h * 64];
  });
#pragma unroll
  for (int h = NH / 2; h < NH; ++h) w[h] = W[h * 64];
#pragma unroll
  for (int tt = 0; tt < 2; ++tt) xv[tt] = *(const u32x2*)(xin + (size_t)(32 * s + 16 * tt + l16) * DM + 16 * ct + 4 * g);
  drain();
  lbar();
  if (tid == 0) set_prog(rc, blockIdx.x * 4, 1u);
  FSP(1)
  if (wave == 0) wait_prog(rc, (blockIdx.x & ~15) * 4, 16, 1, lane, dead, 0);
  lbar();
  const rsrc_t rof = mkrs(ws + WS_OF, OFB);
  u32x4* so = (u32x4*)smem;
  u32x4 ov[4];
#pragma unroll
  for (int n = 0; n < 4; ++n) ov[n] = bld4s(rof, (unsigned)(tid + 512 * n) * 16, (unsigned)s * (NH * 2048));
#pragma unroll
  for (int n = 0; n < 4; ++n) so[tid + 512 * n] = ov[n];
  lbar();
  f32x4 acc[2] = {{0, 0, 0, 0}, {0, 0, 0, 0}};
#pragma unroll
  for (int h = 0; h < NH; ++h)
#pragma unroll
    for (int tt = 0; tt < 2; ++tt) acc[tt] = mfma32(w[h], so[h * 128 + g * 32 + 16 * tt + l16], acc[tt]);
#pragma unroll
  for (int tt = 0; tt < 2; ++tt) {
    const unsigned c0 = cvtpk(bflo(xv[tt].x) + acc[tt][0], bfhi(xv[tt].x) + acc[tt][1]), c1 = cvtpk(bflo(xv[tt].y) + acc[tt][2], bfhi(xv[tt].y) + acc[tt][3]);
    const unsigned n0 = __builtin_amdgcn_permlane16_swap(c0, c0, false, false)[1], n1 = __builtin_amdgcn_permlane16_swap(c1, c1, false, false)[1];
    if (wave < 7 && (g & 1) == 0) *(u32x4*)(xout + (size_t)(32 * s + 16 * tt + l16) * DM + 16 * ct + 4 * g) = u32x4{c0, c1, n0, n1};
  }
  FSP(2)
  if (dead && lane == 0) ((gu32*)(ws + WS_CTL))[CTL_TMO] = 1;
}

#define SLEEP_S 5
#define SLEEP_O 1
#define SLEEP_X 5
#define SLEEP_P 0
constexpr int g_sleepS = SLEEP_S, g_sleepO = SLEEP_O, g_sleepP = SLEEP_P, g_sleepX = SLEEP_X;
extern "C" void kernel_launch(void* const* d_in, const int* in_sizes, int n_in, void* d_out, int out_size, void* d_ws, size_t ws_size, hipStream_t stream) {
  char* ws = (char*)d_ws;
  PrepArgs pa;
  pa.x = (const u16*)d_in[0]; pa.mem0 = (const u16*)d_in[1];
  pa.Wq = (const u16*)d_in[2]; pa.Wk = (const u16*)d_in[3]; pa.Wv = (const u16*)d_in[4]; pa.Wo = (const u16*)d_in[5];
  pa.wq = (const u16*)d_in[8]; pa.wk = (const u16*)d_in[9]; pa.wv = (const u16*)d_in[10]; pa.wo = (const u16*)d_in[11];
  pa.ws = ws;
  constexpr int CHAIN_LDS = 96 * 1024, ATT_LDS = 136 * 1024;
  hipFuncSetAttribute((const void*)k_layer, hipFuncAttributeMaxDynamicSharedMemorySize, CHAIN_LDS);
  hipFuncSetAttribute((const void*)k_fin, hipFuncAttributeMaxDynamicSharedMemorySize, ATT_LDS);
  k_prep<<<256, 512, 0, stream>>>(pa);
  LayerArgs ca;
  ca.ws = ws; ca.x0 = (const u16*)d_in[0];
  ca.sleepS = g_sleepS; ca.sleepO = g_sleepO; ca.sleepP = g_sleepP; ca.sleepX = g_sleepX;
  ca.trace = (unsigned*)(ws + WS_END);
  k_layer<<<256, 512, CHAIN_LDS, stream>>>(ca);
  const int last = NLAYERS - 1;
  k_fin<<<256, 512, ATT_LDS, stream>>>(ws, last, (const u16*)(ws + WS_X1 + last * XB), NLAYERS == 2 ? (u16*)d_out : (u16*)(ws + WS_X2));
}
```
